```python
import torch

def _softmax(x, axis=-1):
    return torch.softmax(x, dim=axis)
def _gelu(x):
    return torch.nn.functional.gelu(x, approximate="tanh")
def _silu(x):
    return torch.nn.functional.silu(x)
def _sigmoid(x):
    return torch.sigmoid(x)
def _layernorm(x, axis=-1, eps=1e-5):
    m = x.mean(dim=axis, keepdim=True)
    v = x.var(dim=axis, keepdim=True, unbiased=False)
    return (x - m) / torch.sqrt(v + eps)
def _scatter_add(tgt, idx, src, axis=0):
    return tgt.clone().index_add_(axis, idx, src)
def _segment_sum(src, seg, n):
    out = torch.zeros((n,) + src.shape[1:], dtype=src.dtype)
    return out.index_add_(0, seg, src)
def _pad(x, pw):
    flat = tuple(v for pair in reversed(pw) for v in pair)
    return torch.nn.functional.pad(x, flat)

kernel_name = 'parallel_sum__sliding_block_cached__geglu_ffn__decode'

def setup_inputs():

    _w = (lambda a: (lambda t: t.to(torch.bfloat16) if t.is_floating_point() else t)(torch.as_tensor(a)))
    def _A_setup():
        B, Sq, D = 4, 1, 640
        Hq, Hkv, Sk, Dh = 32, 32, 8192, 192
        assert Hq % Hkv == 0, (Hq, Hkv)
        return {
            "x": _w((lambda *s: torch.randn(*s, dtype=torch.bfloat16) * 0.05)(B, Sq, D)),
            "k_cache": _w((lambda *s: torch.randn(*s, dtype=torch.bfloat16) * 3.2)(B, Hkv, Sk, Dh)),
            "v_cache": _w((lambda *s: torch.randn(*s, dtype=torch.bfloat16) * 1.6)(B, Hkv, Sk, Dh)),
            "wq": _w((lambda *s: torch.randn(*s, dtype=torch.bfloat16) * 3.2)(D, Hq * Dh) * (D ** -0.5)),
            "wo": _w((lambda *s: torch.randn(*s, dtype=torch.bfloat16) * 1.6)(Hq * Dh, D) * ((Hq * Dh) ** -0.5)),
        }
    _A_in = _A_setup()

    def _B_setup():
        B, S, D, F = 4, 1, 640, 1536
        return {
            "x": _w((lambda *s: torch.randn(*s, dtype=torch.bfloat16) * 0.05)(B, S, D)),
            "w_gate": _w((lambda *s: torch.randn(*s, dtype=torch.bfloat16) * 0.05)(D, F)),
            "w_up": _w((lambda *s: torch.randn(*s, dtype=torch.bfloat16) * 0.05)(D, F)),
            "w_down": _w((lambda *s: torch.randn(*s, dtype=torch.bfloat16) * 0.05)(F, D)),
        }
    _B_in = _B_setup()

    return {
        "A_x": _A_in["x"],
        "A_k_cache": _A_in["k_cache"],
        "A_v_cache": _A_in["v_cache"],
        "A_wq": _A_in["wq"],
        "A_wo": _A_in["wo"],
        "B_x": _B_in["x"],
        "B_w_gate": _B_in["w_gate"],
        "B_w_up": _B_in["w_up"],
        "B_w_down": _B_in["w_down"],
    }

def reference(A_x, A_k_cache, A_v_cache, A_wq, A_wo, B_x, B_w_gate, B_w_up, B_w_down):
    def _A_ref(x, k_cache, v_cache, wq, wo):
        B, Sq, D = x.shape
        Hq, Dh = 32, 192
        Hkv, Sk = k_cache.shape[1], k_cache.shape[2]
        W = min(4096, max(256, Sk // 4))
        rep = Hq // Hkv
        q = (lambda x, *d: x.permute(*d))((x @ wq).reshape(B, Sq, Hq, Dh), 0, 2, 1, 3)
        k = (lambda x, n, axis: torch.repeat_interleave(x, n, dim=axis))(k_cache[:, :, Sk - W:, :], rep, axis=1)
        v = (lambda x, n, axis: torch.repeat_interleave(x, n, dim=axis))(v_cache[:, :, Sk - W:, :], rep, axis=1)
        scores = torch.einsum("bhqd,bhkd->bhqk", q, k) * (Dh ** -0.5)
        p = _softmax(scores, axis=-1)
        o = torch.einsum("bhqk,bhkd->bhqd", p, v)
        return (lambda x, *d: x.permute(*d))(o, 0, 2, 1, 3).reshape(B, Sq, Hq * Dh) @ wo

    def _B_ref(x, w_gate, w_up, w_down):
        return (_gelu(x @ w_gate) * (x @ w_up)) @ w_down

    x = A_x
    xn = _layernorm(x, axis=-1)
    return x + _A_ref(xn, A_k_cache, A_v_cache, A_wq, A_wo) + _B_ref(xn, B_w_gate, B_w_up, B_w_down)
```

```cpp
#include <hip/hip_runtime.h>
#include <hip/hip_bf16.h>
#include <stdint.h>
#define MIMIC 0
constexpr int B=4, D=640, H=32, DH=192, SK=8192, W=2048, F=1536;
typedef unsigned short u16;
__device__ __forceinline__ float bf2f(u16 v){ return __uint_as_float((unsigned)v<<16); }
__device__ __forceinline__ u16 f2bf(float f){ unsigned u=__float_as_uint(f); u += 0x7FFFu + ((u>>16)&1u); return (u16)(u>>16); }
__device__ __forceinline__ float rbf(float f){ return bf2f(f2bf(f)); }
__device__ __forceinline__ float mr(float f){ return MIMIC ? rbf(f) : f; }

__device__ void layernorm(const u16* x, float* xn  , int tid, int nt){
  int w = tid>>6, l = tid&63;
  if (w < 4) {
    float v[10]; float s=0;
    for (int i=0;i<10;i++){ v[i]=bf2f(x[w*D + i*64 + l]); s+=v[i]; }
    for (int o=32;o;o>>=1) s += __shfl_xor(s,o);
    float mean = s*(1.0f/D); float q=0;
    for (int i=0;i<10;i++){ float d=v[i]-mean; q+=d*d; }
    for (int o=32;o;o>>=1) q += __shfl_xor(q,o);
    float var = q*(1.0f/D);
    float mb = mr(mean), vb = mr(var);
    float den = mr(sqrtf(mr(vb + 1e-5f)));
    for (int i=0;i<10;i++){ xn[w*D + i*64 + l] = mr(mr(v[i]-mb)/den); }
  }
  __syncthreads();
}
__global__ void k1(const u16* x, const u16* wq, const u16* wg, const u16* wu, float* q, float* hh){
  __shared__ float xn[B*D];
  layernorm(x, xn, threadIdx.x, blockDim.x);
  int col = blockIdx.x*256 + threadIdx.x;
  if (col < H*DH) {
    float a[4]={0,0,0,0};
    for (int k=0;k<D;k++){ float w=bf2f(wq[(size_t)k*H*DH+col]); for(int b=0;b<4;b++) a[b]=fmaf(xn[b*D+k],w,a[b]); }
    for(int b=0;b<4;b++) q[b*H*DH+col]=mr(a[b]);
  } else {
    int f = col - H*DH;
    float g[4]={0,0,0,0},u[4]={0,0,0,0};
    for (int k=0;k<D;k++){ float w=bf2f(wg[(size_t)k*F+f]), w2=bf2f(wu[(size_t)k*F+f]); for(int b=0;b<4;b++){ g[b]=fmaf(xn[b*D+k],w,g[b]); u[b]=fmaf(xn[b*D+k],w2,u[b]);} }
    for(int b=0;b<4;b++){ float gg=mr(g[b]); float t=0.5f*gg*(1.0f+tanhf(0.7978845608028654f*(gg+0.044715f*gg*gg*gg))); hh[b*F+f]=mr(mr(t)*mr(u[b])); }
  }
}
__global__ void __launch_bounds__(1024) k2(const u16* kc, const u16* vc, const float* q, float* o){
  __shared__ float sc[W]; __shared__ float qs[DH]; __shared__ float red[16]; __shared__ float oacc[5][DH];
  int bh = blockIdx.x, tid=threadIdx.x;
  if (tid<DH) qs[tid]=q[bh*DH+tid];
  __syncthreads();
  const u16* kb = kc + ((size_t)bh*SK + (SK-W))*DH; const u16* vb = vc + ((size_t)bh*SK + (SK-W))*DH;
  float m=-INFINITY;
  for (int j=tid;j<W;j+=1024){ float s=0; for(int d=0;d<DH;d++) s=fmaf(qs[d],bf2f(kb[(size_t)j*DH+d]),s); s=mr(mr(s)*0.07216878364870322f); sc[j]=s; m=fmaxf(m,s);}
  for (int of=32;of;of>>=1) m=fmaxf(m,__shfl_xor(m,of));
  if((tid&63)==0) red[tid>>6]=m; __syncthreads();
  m=red[0]; for(int i=1;i<16;i++) m=fmaxf(m,red[i]); __syncthreads();
  float l=0; for (int j=tid;j<W;j+=1024){ float p=expf(sc[j]-m); sc[j]=p; l+=p; }
  for (int of=32;of;of>>=1) l+=__shfl_xor(l,of);
  if((tid&63)==0) red[tid>>6]=l; __syncthreads();
  l=0; for(int i=0;i<16;i++) l+=red[i];
  if (tid<960){ int d=tid%DH, g=tid/DH; float a=0; for(int j=g;j<W;j+=5){ float p = (MIMIC>=2)? rbf(sc[j]/l) : sc[j]/l; a=fmaf(p,bf2f(vb[(size_t)j*DH+d]),a);} oacc[g][d]=a; }
  __syncthreads();
  if (tid<DH){ float a=0; for(int g=0;g<5;g++) a+=oacc[g][tid]; o[bh*DH+tid]=mr(a); }
}
__global__ void k3(const u16* x, const float* o, const float* hh, const u16* wo, const u16* wd, u16* out){
  __shared__ float ra[16][64], rb[16][64];
  int b = blockIdx.x/10, c = (blockIdx.x%10)*64 + (threadIdx.x&63), w = threadIdx.x>>6;
  float a=0, f=0;
  for (int k=w;k<H*DH;k+=16) a=fmaf(o[b*H*DH+k],bf2f(wo[(size_t)k*D+c]),a);
  for (int k=w;k<F;k+=16) f=fmaf(hh[b*F+k],bf2f(wd[(size_t)k*D+c]),f);
  ra[w][threadIdx.x&63]=a; rb[w][threadIdx.x&63]=f; __syncthreads();
  if (w==0){ a=0;f=0; for(int i=0;i<16;i++){a+=ra[i][threadIdx.x]; f+=rb[i][threadIdx.x];}
    float r = (MIMIC>=2) ? rbf(bf2f(x[b*D+c])+rbf(a))+rbf(f) : bf2f(x[b*D+c])+a+f; out[b*D+c]=f2bf(r); }
}
extern "C" void kernel_launch(void* const* d_in, const int* in_sizes, int n_in, void* d_out, int out_size, void* d_ws, size_t ws_size, hipStream_t stream){
  const u16* x=(const u16*)d_in[0]; const u16* kc=(const u16*)d_in[1]; const u16* vc=(const u16*)d_in[2];
  const u16* wq=(const u16*)d_in[3]; const u16* wo=(const u16*)d_in[4]; const u16* wg=(const u16*)d_in[6]; const u16* wu=(const u16*)d_in[7]; const u16* wd=(const u16*)d_in[8];
  float* q=(float*)d_ws; float* hh=q+B*H*DH; float* o=hh+B*F;
  k1<<<(H*DH+F)/256,256,0,stream>>>(x,wq,wg,wu,q,hh);
  k2<<<B*H,1024,0,stream>>>(kc,vc,q,o);
  k3<<<40,1024,0,stream>>>(x,o,hh,wo,wd,(u16*)d_out);
}
```

```cpp
#define KMAP 8
#define SPRIO 1
#define NCK 0
#define W2E 4
#define BOOST 1
#define HOLD 24
#define CPF 18
#include <hip/hip_runtime.h>
#include <stdint.h>
#include <type_traits>

#define GAS __attribute__((address_space(1)))
#define LAS __attribute__((address_space(3)))
#define DI __device__ __forceinline__
typedef unsigned u32;
typedef unsigned long long u64;
typedef unsigned short u16;
typedef u32 v2u __attribute__((ext_vector_type(2)));
typedef u32 v4u __attribute__((ext_vector_type(4)));
typedef float v4f __attribute__((ext_vector_type(4)));
typedef float v2f __attribute__((ext_vector_type(2)));
typedef short v4s __attribute__((ext_vector_type(4)));
typedef short v8s __attribute__((ext_vector_type(8)));
typedef __bf16 bf2 __attribute__((ext_vector_type(2)));
typedef __amdgpu_buffer_rsrc_t rsrc_t;

#define P_RAMP 3
#define P_RUN 3
#define P_FIRST 1
#define SLOW0 5
#define P_END 0
#define TG 0
#define BK 6
#define B0 9
#define TK 0
#define T0 6
#define NS 16
#define BK1 5
#define B01 9
#define TK1 0
#define T01 6
#define TOUCH 1
#define OUT_AUX 0
#define PRIO 3
#define AUX_KV 2
#define AUX_A 2
#define AUX_W2 2
#define STAMP 0

constexpr int NB = 4, D = 640, H = 32, DH = 192, SK = 8192, WIN = 2048, F = 1536;
constexpr int NSW = 8, NHW = 4, NT = (NSW + NHW) * 64, NWG = 256;
constexpr int STEPS = WIN / 2 / (NSW * 8);
constexpr int STEP_B = (KMAP == 8 ? 16 : KMAP == 7 ? 32 : KMAP == 10 ? 128 : 64) * DH * 2;
constexpr int BSTART = 54, CSTART = BOOST ? (NCK ? NCK + 5 : BSTART) : 1000;
constexpr int NLD = 2 * STEPS * 3, LANDED = HOLD - P_RAMP - (CSTART < HOLD ? 1 : 0);
constexpr int issued(int k) {
  const int p = P_RUN + (k >= NLD - 16 ? P_END : 0) + (k >= CSTART ? 1 : 0);
  const int e = HOLD + (k < LANDED ? k : LANDED) / 6, t = k + p > e ? k + p : e;
  return t < NLD ? t : NLD;
}
constexpr unsigned SPIN_CAP = 1u << 17;
constexpr int G_E = 0, G_Q = 16, G_G = G_Q + H * DH, G_U = G_G + F, G_ML = G_U + F, G_CK = G_ML + NWG * 8, G_O = G_CK + NWG * 8, G_Y = G_O + NWG * 48,
              G_END = G_Y + 80 * 256;
constexpr int G_STAMP = 1 << 20;
constexpr int W2_ROW = 288, SLAB_BLK = 1088, L_W2 = 0, L_SLAB = 0, L_AOP = 80 * SLAB_BLK, AOP_ROW = 576, L_XN = L_AOP + 4 * AOP_ROW, XN_ROW = 1344,
              L_Q = L_XN + 4 * XN_ROW, L_OP = L_Q + DH * 2, L_ML = L_OP + NSW * DH * 4, L_F = L_ML + NSW * 8,
              L_TAG = L_F + NSW * 4, L_FA = L_TAG + 16, L_FQ = L_FA + NHW * 4, L_FW = L_FQ + NHW * 4, L_FML = L_FW + NHW * 4, L_FV = L_FML + NSW * 4,
              L_FC = L_FV + NSW * 4, L_BOOST = L_FC + 8, L_END = L_BOOST + 8;
static_assert(256 * W2_ROW <= L_AOP, "the wo / w_down image lies in the place of the slab");

DI float bf_lo(u32 w) { return __uint_as_float(w << 16); }
DI float bf_hi(u32 w) { return __uint_as_float(w & 0xffff0000u); }
DI u32 bf_bits(float f) { return (u32)__builtin_bit_cast(u16, (__bf16)f); }
DI float bf_round(float f) { return __uint_as_float(bf_bits(f) << 16); }
DI u32 bf_pack(float lo, float hi) { return __builtin_bit_cast(u32, bf2{(__bf16)lo, (__bf16)hi}); }
DI float dot2(u32 a, u32 b, float c) {
  return __builtin_amdgcn_fdot2_f32_bf16(__builtin_bit_cast(bf2, a), __builtin_bit_cast(bf2, b), c, false);
}
template <int CTRL> DI float dpp(float x) {
  return __builtin_bit_cast(float, __builtin_amdgcn_mov_dpp(__builtin_bit_cast(int, x), CTRL, 0xf, 0xf, true));
}
constexpr int XOR1 = 0xB1, XOR2 = 0x4E, HMIRROR = 0x141, ROR8 = 0x128;
DI float sum8(float x) { x += dpp<XOR1>(x); x += dpp<XOR2>(x); return x + dpp<HMIRROR>(x); }
DI float fold32(float a, float b) {
  auto s = __builtin_amdgcn_permlane32_swap(__float_as_uint(a), __float_as_uint(b), false, false);
  return __uint_as_float(s[0]) + __uint_as_float(s[1]);
}
DI float fold16(float a, float b) {
  auto s = __builtin_amdgcn_permlane16_swap(__float_as_uint(a), __float_as_uint(b), false, false);
  return __uint_as_float(s[0]) + __uint_as_float(s[1]);
}
DI float fold8(float a, float b, bool up) {
  const float t1 = a + dpp<ROR8>(a), t2 = b + dpp<ROR8>(b);
  return up ? t2 : t1;
}
DI float max16_32(float x) {
  auto s = __builtin_amdgcn_permlane16_swap(__float_as_uint(x), __float_as_uint(x), false, false);
  x = fmaxf(__uint_as_float(s[0]), __uint_as_float(s[1]));
  auto t = __builtin_amdgcn_permlane32_swap(__float_as_uint(x), __float_as_uint(x), false, false);
  return fmaxf(__uint_as_float(t[0]), __uint_as_float(t[1]));
}
#define CAS __attribute__((address_space(4)))
DI v8s tr_pair(LAS unsigned char* p, int second) {
  const v4s b0 = __builtin_amdgcn_ds_read_tr16_b64_v4i16((LAS v4s*)p);
  const v4s b1 = __builtin_amdgcn_ds_read_tr16_b64_v4i16((LAS v4s*)(p + second));
  return __builtin_shufflevector(b0, b1, 0, 1, 2, 3, 4, 5, 6, 7);
}
DI v4f mfma(v4u a, v8s b, v4f c) { return __builtin_amdgcn_mfma_f32_16x16x32_bf16(__builtin_bit_cast(v8s, a), b, c, 0, 0, 0); }
DI rsrc_t make_rsrc(const void* p, int bytes) { return __builtin_amdgcn_make_buffer_rsrc((void*)p, 0, bytes, 0x00020000); }
#define PIN() __builtin_amdgcn_sched_barrier(0)
#define WAIT_VM(n) do { PIN(); asm volatile("s_waitcnt vmcnt(%0)" ::"n"(n) : "memory"); PIN(); } while (0)
#define BAR() do { PIN(); asm volatile("s_waitcnt lgkmcnt(0)\n\ts_barrier" ::: "memory"); PIN(); } while (0)
template <int N> DI void wait_vm() { WAIT_VM(N); }
template <int N, int W, bool SLOW> DI void pace(int fast, int slow) {
  PIN();
  asm volatile("s_cmp_gt_i32 %0, %1\n\ts_cbranch_scc1 1f\n\ts_waitcnt vmcnt(%2)\n1:" ::"s"(fast), "n"(N), "n"(W) : "scc", "memory");
  if constexpr (SLOW && W >= 1) asm volatile("s_cmp_le_i32 %0, %1\n\ts_cbranch_scc1 1f\n\ts_waitcnt vmcnt(%2)\n1:" ::"s"(slow), "n"(N), "n"(W - 1) : "scc", "memory");
  PIN();
}
template <int I, int E, class Fn> DI void static_for(Fn&& fn) {
  if constexpr (I < E) { fn(std::integral_constant<int, I>{}); static_for<I + 1, E>(fn); }
}
#define RLX_AGENT __ATOMIC_RELAXED, __HIP_MEMORY_SCOPE_AGENT

constexpr int SC1 = 16;
DI void put4(rsrc_t rw, int g, v4u tag, v4f v) {
  v4u o;
#pragma unroll
  for (int k = 0; k < 4; ++k) o[k] = ((__float_as_uint(v[k]) + 0x200u) & 0xfffffc00u) | tag[k];
  __builtin_amdgcn_raw_buffer_store_b128(o, rw, g * 16, 0, SC1);
}
DI bool stale(v4u x, v4u tag) { return ((((x[0] ^ tag[0]) | (x[1] ^ tag[1])) | ((x[2] ^ tag[2]) | (x[3] ^ tag[3]))) & 0x3ffu) != 0; }
template <int N, int SLEEP> DI void get4(rsrc_t rw, u64 on, int g, int stride, v4u tag, v4f (&v)[N]) {
  v4u x[N];
  u64 pend[N];
#pragma unroll
  for (int n = 0; n < N; ++n) { pend[n] = on; x[n] = v4u{0u, 0u, 0u, 0u}; }
#pragma unroll 1
  for (u32 spins = 0; spins < SPIN_CAP; ++spins) {
    asm volatile("" ::: "memory");
#pragma unroll
    for (int n = 0; n < N; ++n)
      if (__builtin_amdgcn_inverse_ballot_w64(pend[n])) x[n] = __builtin_amdgcn_raw_buffer_load_b128(rw, g * 16, n * stride * 16, SC1);
    u64 any = 0;
#pragma unroll
    for (int n = 0; n < N; ++n) { pend[n] &= __builtin_amdgcn_ballot_w64(stale(x[n], tag)); any |= pend[n]; }
    if (any == 0) break;
    if (SLEEP) __builtin_amdgcn_s_sleep(SLEEP);
  }
#pragma unroll
  for (int n = 0; n < N; ++n)
#pragma unroll
    for (int k = 0; k < 4; ++k) v[n][k] = __uint_as_float(x[n][k] & 0xfffffc00u);
}
constexpr u64 ALL = ~0ull, L48 = (1ull << 48) - 1;
DI void lds_arrive(LAS unsigned char* p) {
  asm volatile("s_waitcnt lgkmcnt(0)" ::: "memory");
  *(volatile LAS u32*)p = 1u;
}
template <int SLEEP, int N> DI void lds_wait(LAS unsigned char* p, int lane) {
  while (!__all(*(volatile LAS u32*)(p + (lane & (N - 1)) * 4) != 0u)) __builtin_amdgcn_s_sleep(SLEEP);
  asm volatile("" ::: "memory");
}
#define ST(i) do {} while (0)
#define STW(lo, hi) do {} while (0)

__global__ void __launch_bounds__(NT, 3)
fused_decode(const u16* __restrict__ x_, const u16* __restrict__ kc, const u16* __restrict__ vc, const u16* __restrict__ wq_,
             const u16* __restrict__ wo_, const u16* __restrict__ wg_, const u16* __restrict__ wu_, const u16* __restrict__ wd_,
             u16* __restrict__ out_, u64* ws_) {
  __shared__ __attribute__((aligned(16))) unsigned char smem[L_END];
  LAS unsigned char* L = (LAS unsigned char*)smem;
  asm volatile("" ::"s"(x_), "s"(kc), "s"(vc), "s"(wq_), "s"(wo_), "s"(wg_), "s"(wu_), "s"(wd_), "s"(out_), "s"(ws_));
  const GAS u16* x = (const GAS u16*)x_;
  const rsrc_t rw = make_rsrc(ws_, G_END * 16);
  const int tid = threadIdx.x, lane = tid & 63, w = __builtin_amdgcn_readfirstlane(tid >> 6);
  const int r = lane >> 3, c = lane & 7;
  const int bid = blockIdx.x, j = bid >> 3;
  const int h = (bid & 7) * 4 + (j >> 3), sub = j & 7, b = sub >> 1, wg = h * 8 + sub;
  const int a_lim = (h & 3) < 3 ? 5 : 3;
  const bool has_a = sub < a_lim;
  constexpr float LOG2E = 1.4426950408889634f;
  v4u tag;

  const size_t base = ((size_t)(b * H + h) * SK + (SK - WIN)) * DH;
  const rsrc_t rk = make_rsrc(kc + base, WIN * DH * 2), rv = make_rsrc(vc + base, WIN * DH * 2);
  const int key0 = KMAP == 8 ? 1024 * (sub & 1) + 256 * (w >> 1) + 8 * (w & 1) : KMAP == 7 ? 1024 * (sub & 1) + 512 * (w >> 2) + 8 * (w & 3)
                 : KMAP == 10 ? 64 * (sub & 1) + 8 * w : 1024 * (sub & 1) + 8 * w;
  const int voff = (key0 + r) * (DH * 2) + c * 16;
  const int hw = w - NSW;
  v4u first;
  u64 e0;
  u32 x2;
  v4u cpf;
  ST(0);
  if (w < NSW) {
    first = __builtin_amdgcn_raw_buffer_load_b128(rk, voff, 0, AUX_KV);
  } else {
    e0 = __hip_atomic_load((GAS u64*)ws_ + G_E, RLX_AGENT);
    first = ((const GAS v4u*)x)[hw * (D / 8) + lane];
    x2 = ((const GAS u32*)x)[hw * (D / 2) + 256 + lane];
    {
      u64 pc; asm volatile("s_getpc_b64 %0" : "=s"(pc));
      cpf = __builtin_amdgcn_raw_buffer_load_b128(make_rsrc((const void*)(pc & ~(u64)255), CPF * 1024), (j * 4 + hw) * 256 + (lane & 15) * 16, 0, 0);
    }
    if (lane < 3 * NHW + 2 * NSW + 4 && hw == 0) *(LAS u32*)(L + L_FA + lane * 4) = 0u;
    if (lane < 8) *(LAS u32*)(L + L_AOP + hw * AOP_ROW + 240 * 2 + lane * 4) = 0u;
  }
  BAR();

  if (w < NSW) {
    v4u buf[NLD];
#define ISSUE(n) buf[n] = __builtin_amdgcn_raw_buffer_load_b128((n) < 3 * STEPS ? rk : rv, voff + ((n) % 3) * 128, (((n) / 3) % STEPS) * STEP_B, AUX_KV)
    buf[0] = first;
    if (w >= 4) __builtin_amdgcn_s_setprio(SPRIO);
    int fast = 0, slow = 0;
    auto read_pace = [&]() {
      const v2u t = *(volatile LAS v2u*)(L + L_BOOST);
      fast = __builtin_amdgcn_readfirstlane(t[0]);
      slow = __builtin_amdgcn_readfirstlane(t[1]);
    };
    const u16* tp = w == 0 ? vc + base : w == 1 ? vc + base + (WIN * DH - 2) : w == 2 ? kc + base + (WIN * DH - 2) : w == 3 ? wo_ + h * DH * D
                  : w == 4 ? wo_ + (h + 1) * DH * D - 2 : w == 5 ? wd_ + h * 48 * D : w == 6 ? wd_ + (h + 1) * 48 * D - 2 : out_;
    const u32 touched = *(const CAS u32*)tp;
    static_for<1, HOLD>([&](auto N_) {
      constexpr int n = N_;
      if constexpr (n == 1) wait_vm<0>();
      else if constexpr (n > P_RAMP && n < CSTART) wait_vm<P_RAMP - 1>();
      if constexpr (BOOST && NCK && n == NCK) *(volatile LAS u32*)(L + L_FC + (w ? 4 : 0)) = 1u;
      if constexpr (n == CSTART) read_pace();
      if constexpr (n >= CSTART) { wait_vm<P_RAMP>(); pace<n, P_RAMP - 1, TK1 != 0>(fast, slow); }
      if constexpr (n > 1 && n <= SLOW0) {
        PIN();
        asm volatile("s_cmp_gt_i32 %0, 0\n\ts_cbranch_scc1 1f\n\ts_waitcnt vmcnt(0)\n1:" ::"s"(a_lim - sub) : "scc", "memory");
        PIN();
      }
      ISSUE(n);
    });
    asm volatile("" ::"s"(touched));
    ST(1);
    lds_wait<1, NHW>(L + L_FQ, lane);
    ST(2);
    v4u q[3];
#pragma unroll
    for (int i = 0; i < 3; ++i) q[i] = *(LAS v4u*)(L + L_Q + i * 128 + c * 16);
    float sc[STEPS], acc[24];
    static_for<0, NLD>([&](auto N_) {
      constexpr int n = N_, s = n / 3;
      if constexpr (n >= LANDED) wait_vm<issued(n) - 1 - n>();
      if constexpr (BOOST && (n == BSTART - 1 || n == BSTART + 5 || (NCK && n == HOLD))) read_pace();
      if constexpr (n >= CSTART - 1 && n >= LANDED && issued(n + 1) > issued(n) && issued(n) - 2 - n >= 0) pace<n, issued(n) - 2 - n, TK != 0 || TK1 != 0>(fast, slow);
      static_for<issued(n), issued(n + 1)>([&](auto K_) { constexpr int k = K_; ISSUE(k); });
      if constexpr (issued(n) < NLD && issued(n + 1) == NLD) lds_arrive(L + L_FV + w * 4);
      PIN();
      if constexpr (n % 3 == 2 && s < STEPS) {
        float d0 = 0.f, d1 = 0.f, d2 = 0.f;
#pragma unroll
        for (int e = 0; e < 4; ++e) {
          d0 = dot2(buf[3 * s][e], q[0][e], d0);
          d1 = dot2(buf[3 * s + 1][e], q[1][e], d1);
          d2 = dot2(buf[3 * s + 2][e], q[2][e], d2);
        }
        sc[s] = bf_round(bf_round(sum8(d0 + d1 + d2)) * 0.07216878364870322f);
        asm volatile("" : "+v"(sc[s]));
      }
      if constexpr (n == 3 * STEPS - 1) {
        float m = sc[0];
#pragma unroll
        for (int t = 1; t < STEPS; ++t) m = fmaxf(m, sc[t]);
        m = max16_32(fmaxf(m, dpp<ROR8>(m)));
        float l = 0.f;
#pragma unroll
        for (int t = 0; t < STEPS; ++t) { sc[t] = __builtin_amdgcn_exp2f((sc[t] - m) * LOG2E); l += sc[t]; }
        l += dpp<ROR8>(l);
        l = fold16(l, l);
        l = fold32(l, l);
        *(LAS v2f*)(L + L_ML + w * 8) = v2f{m, l};
        lds_arrive(L + L_FML + w * 4);
        tag = *(LAS v4u*)(L + L_TAG);
        ST(3);
#pragma unroll
        for (int i = 0; i < 24; ++i) acc[i] = 0.f;
      }
      if constexpr (s >= STEPS) {
        constexpr int i = n % 3;
#pragma unroll
        for (int e = 0; e < 4; ++e) {
          const u32 v = buf[n][e];
          acc[8 * i + 2 * e] = fmaf(sc[s - STEPS], bf_lo(v), acc[8 * i + 2 * e]);
          acc[8 * i + 2 * e + 1] = fmaf(sc[s - STEPS], bf_hi(v), acc[8 * i + 2 * e + 1]);
          asm volatile("" : "+v"(acc[8 * i + 2 * e]), "+v"(acc[8 * i + 2 * e + 1]));
        }
        if constexpr (s == 2 * STEPS - 1) {
          float* a = acc + 8 * i;
          const float t = fold8(fold16(fold32(a[0], a[4]), fold32(a[2], a[6])), fold16(fold32(a[1], a[5]), fold32(a[3], a[7])), r & 1);
          *(LAS float*)(L + L_OP + (w * DH + 64 * i + 8 * c + r) * 4) = t;
        }
      }
      PIN();
    });
#undef ISSUE
    ST(4);
    BAR();
    ST(5);
    if (w == 0 && lane < 48) {
      const v4f f0 = *(LAS v4f*)(L + L_F), f1 = *(LAS v4f*)(L + L_F + 16);
      v4f p[NSW];
#pragma unroll
      for (int k = 0; k < NSW; ++k) p[k] = *(LAS v4f*)(L + L_OP + k * DH * 4 + lane * 16);
      PIN();
      const v4f o = ((f0[0] * p[0] + f0[1] * p[1]) + (f0[2] * p[2] + f0[3] * p[3])) + ((f1[0] * p[4] + f1[1] * p[5]) + (f1[2] * p[6] + f1[3] * p[7]));
      put4(rw, G_O + wg * 48 + lane, tag, o);
    }
    if (sub >= 3) {
      const int g = lane >> 4;
      v8s bf[8];
      v4u af[8];
      lds_wait<1, NHW>(L + L_FW, lane);
#pragma unroll
      for (int ks = 0; ks < 8; ++ks) bf[ks] = tr_pair(L + L_W2 + (32 * ks + 8 * g + ((lane & 15) >> 2)) * W2_ROW + (16 * w + 4 * (lane & 3)) * 2, 4 * W2_ROW);
      LAS unsigned char* ap = L + L_AOP + (lane & 3) * AOP_ROW + 16 * g;
      v4f y = mfma(*(LAS v4u*)(ap + 7 * 64), bf[7], mfma(*(LAS v4u*)(ap + 6 * 64), bf[6], v4f{0.f, 0.f, 0.f, 0.f}));
      asm volatile("" : "+v"(y), "+v"(bf[0]), "+v"(bf[1]), "+v"(bf[2]), "+v"(bf[3]), "+v"(bf[4]), "+v"(bf[5]));
      BAR();
      ST(6);
#pragma unroll
      for (int ks = 0; ks < 6; ++ks) af[ks] = *(LAS v4u*)(ap + ks * 64);
      PIN();
#pragma unroll
      for (int ks = 0; ks < 6; ++ks) y = mfma(af[ks], bf[ks], y);
      if (lane < 16) put4(rw, G_Y + (((sub - 3) * 16 + 2 * w + r) * 32 + h) * 8 + c, tag, y);
      ST(7);
    } else if (w == 5 && h * 3 + sub < 80) {
      const int ocol = 8 * (h * 3 + sub) + c, ob = lane >> 4;
      const u32 xres = x[ob * D + ocol];
      v4f yv[4];
      get4<4, 0>(rw, ALL, G_Y + (h * 3 + sub) * 256 + lane, 64, tag, yv);
      const v4f s4 = (yv[0] + yv[1]) + (yv[2] + yv[3]);
      float s = fold16(fold32(s4[0], s4[2]), fold32(s4[1], s4[3]));
      s += dpp<ROR8>(s);
      s += bf_lo(xres);
      const u32 mine = bf_bits(s), other = (u32)__builtin_amdgcn_mov_dpp((int)mine, XOR1, 0xf, 0xf, true);
      if ((lane & 9) == 0) __builtin_amdgcn_raw_buffer_store_b32(mine | (other << 16), make_rsrc(out_, NB * D * 2), (ob * D + ocol) * 2, 0, OUT_AUX);
      if (bid == 0 && lane == 0) {
        const u64 epoch = (u64)tag[0] | ((u64)tag[1] << 10) | ((u64)((tag[2] - tag[0]) & 0x3ffu) << 20) | ((u64)tag[3] << 30);
        __hip_atomic_store((GAS u64*)ws_ + G_E, epoch, RLX_AGENT);
      }
      ST(13);
    }
  } else {
    __builtin_amdgcn_s_setprio(PRIO);
    const int acol = sub < 3 ? h * DH + 64 * sub : (3 * (h >> 2) + (h & 3)) * 64, astr = sub < 3 ? H * DH : F;
    auto make_tag = [&]() {
      asm volatile("" : "+v"(e0));
      const u64 epoch = e0 + 1;
      tag = v4u{(u32)epoch & 0x3ffu, (u32)(epoch >> 10) & 0x3ffu, (u32)((epoch >> 20) + epoch) & 0x3ffu, (u32)(epoch >> 30) & 0x3ffu};
      *(LAS v4u*)(L + L_TAG) = tag;
    };
    if (!has_a) {
      make_tag();
    } else {
      v4u ab[20];
      const rsrc_t ra = make_rsrc((sub < 3 ? wq_ : sub == 3 ? wg_ : wu_) + hw * 160 * astr + acol, 159 * astr * 2 + 128);
#pragma unroll
      for (int i = 0; i < 20; ++i) ab[i] = __builtin_amdgcn_raw_buffer_load_b128(ra, r * astr * 2 + c * 16, i * 16 * astr, AUX_A);
      PIN();
      make_tag();
      float v[10], s = 0.f;
#pragma unroll
      for (int i = 0; i < 5; ++i) { const u32 t = i < 4 ? first[i & 3] : x2; v[2 * i] = bf_lo(t); v[2 * i + 1] = bf_hi(t); s += v[2 * i] + v[2 * i + 1]; }
      s = sum8(s); s += dpp<ROR8>(s); s = fold16(s, s); s = fold32(s, s);
      const float mean = s * (1.0f / D);
      float qq = 0.f;
#pragma unroll
      for (int i = 0; i < 10; ++i) qq = fmaf(v[i] - mean, v[i] - mean, qq);
      qq = sum8(qq); qq += dpp<ROR8>(qq); qq = fold16(qq, qq); qq = fold32(qq, qq);
      const float mb = bf_round(mean), inv = __builtin_amdgcn_rcpf(bf_round(__builtin_amdgcn_sqrtf(bf_round(bf_round(qq * (1.0f / D)) + 1e-5f))));
      u32 xn[5];
#pragma unroll
      for (int i = 0; i < 5; ++i) xn[i] = bf_pack(bf_round(v[2 * i] - mb) * inv, bf_round(v[2 * i + 1] - mb) * inv);
      *(LAS v4u*)(L + L_XN + hw * XN_ROW + lane * 16) = v4u{xn[0], xn[1], xn[2], xn[3]};
      *(LAS u32*)(L + L_XN + hw * XN_ROW + 1024 + lane * 4) = xn[4];
      ST(14);
      PIN();
#pragma unroll
      for (int i = 0; i < 20; ++i) *(LAS v4u*)(L + L_SLAB + (hw * 20 + i) * SLAB_BLK + r * 128 + (c ^ (r & 2)) * 16) = ab[i];
      lds_arrive(L + L_FA + hw * 4);
      lds_wait<1, NHW>(L + L_FA, lane);
      ST(9);
      const int g = lane >> 4, tq = (lane & 15) >> 2;
      v4f a4 = {0.f, 0.f, 0.f, 0.f};
#pragma unroll
      for (int ks = 0; ks < 20; ++ks)
        a4 = mfma(*(LAS v4u*)(L + L_XN + (lane & 3) * XN_ROW + (32 * ks + 8 * g) * 2),
                  tr_pair(L + L_SLAB + (4 * ks + g) * SLAB_BLK + tq * 128 + ((2 * hw + ((lane & 3) >> 1)) ^ (tq & 2)) * 16 + (lane & 1) * 8, 512), a4);
      if (lane < 16) put4(rw, (sub < 3 ? G_Q : sub == 3 ? G_G : G_U) + acol + 16 * hw + lane, tag, v4f{bf_round(a4[0]), bf_round(a4[1]), bf_round(a4[2]), bf_round(a4[3])});
      ST(8);
    }
    asm volatile("" ::"v"(cpf));
    if (hw < 3) {
      v4f qv[1];
      get4<1, 2>(rw, ALL, G_Q + h * DH + hw * 64 + lane, 0, tag, qv);
      const float qb = b == 0 ? qv[0][0] : b == 1 ? qv[0][1] : b == 2 ? qv[0][2] : qv[0][3];
      *(LAS u16*)(L + L_Q + (hw * 64 + lane) * 2) = (u16)(__float_as_uint(qb) >> 16);
      ST(10);
    }
    lds_arrive(L + L_FQ + hw * 4);
    auto set_pace = [&](int g, int from, int b0, int bk, int t0, int tk) {
      const v4u t = __builtin_amdgcn_raw_buffer_load_b128(rw, lane < NS ? (g + ((wg + 3 + (256 / NS) * lane + (lane & (256 / NS / 2 - 1))) & 255) * 8) * 16 : 0x40000000, 0, SC1);
      const int cnt = __builtin_popcountll(__builtin_amdgcn_ballot_w64(lane < NS && !stale(t, tag)));
      *(volatile LAS v2u*)(L + L_BOOST) = v2u{(u32)(cnt > b0 ? from + (cnt - b0) * bk : 0), (u32)(cnt < t0 ? from + (t0 - cnt) * tk : 0)};
    };
    if (BOOST && NCK && hw == 3) {
      lds_wait<2, 1>(L + L_FC, lane);
      if (lane == 0) put4(rw, G_CK + wg * 8, tag, v4f{0.f, 0.f, 0.f, 0.f});
      set_pace(G_CK, CSTART, B01, BK1, T01, TK1);
    }
    if (hw == 0) {
      lds_wait<4, NSW>(L + L_FML, lane);
      const v2f ml = *(LAS v2f*)(L + L_ML + c * 8);
      float M = fmaxf(ml[0], dpp<XOR1>(ml[0]));
      M = fmaxf(M, dpp<XOR2>(M));
      M = fmaxf(M, dpp<HMIRROR>(M));
      const float Ls = sum8(ml[1] * __builtin_amdgcn_exp2f((ml[0] - M) * LOG2E));
      const float Lh = __uint_as_float(__float_as_uint(Ls) & 0xfffffc00u);
      if (lane == 0) put4(rw, G_ML + wg * 8, tag, v4f{M, Lh, Ls - Lh, 0.f});
      if (BOOST) set_pace(G_ML, BSTART, B0, BK, T0, TK);
      v4f o2[1];
      get4<1, 16>(rw, ALL, G_ML + (wg ^ 1) * 8, 0, tag, o2);
      const float Mt = fmaxf(M, o2[0][0]);
      const float Lt = Ls * __builtin_amdgcn_exp2f((M - Mt) * LOG2E) + (o2[0][1] + o2[0][2]) * __builtin_amdgcn_exp2f((o2[0][0] - Mt) * LOG2E);
      if (lane < NSW) *(LAS float*)(L + L_F + lane * 4) = __builtin_amdgcn_exp2f((ml[0] - Mt) * LOG2E) / Lt;
      ST(11);
    } else if (hw == 1 && sub >= 3) {
      v4f gu[2];
      get4<2, 16>(rw, L48, G_G + 48 * h + lane, F, tag, gu);
#pragma unroll 1
      for (int bb = 0; bb < 4; ++bb) {
        const float gt = gu[0][0], ut = gu[1][0];
        const float ge = 0.5f * gt * (1.0f + tanhf(0.7978845608028654f * (gt + 0.044715f * gt * gt * gt)));
        if (lane < 48) *(LAS u16*)(L + L_AOP + bb * AOP_ROW + (DH + lane) * 2) = (u16)bf_bits(bf_round(ge) * ut);
        gu[0] = v4f{gu[0][1], gu[0][2], gu[0][3], 0.f};
        gu[1] = v4f{gu[1][1], gu[1][2], gu[1][3], 0.f};
      }
    }
    v4u wb[15];
    if (sub >= 3) {
      const rsrc_t ro = make_rsrc(wo_ + h * DH * D + 128 * (sub - 3), (DH - 1) * D * 2 + 256), rd = make_rsrc(wd_ + h * 48 * D + 128 * (sub - 3), 47 * D * 2 + 256);
      lds_wait<4, NHW>(L + L_FQ, lane);
#pragma unroll 1
      for (int i = 0; i < W2E; ++i)
        *(LAS v4u*)(L + L_W2 + (16 * i + 4 * hw + (lane >> 4)) * W2_ROW + (lane & 15) * 16) =
            __builtin_amdgcn_raw_buffer_load_b128(ro, (4 * hw + (lane >> 4)) * (D * 2) + (lane & 15) * 16, i * (16 * D * 2), AUX_W2);
      lds_wait<2, NSW>(L + L_FV, lane);
#pragma unroll
      for (int i = W2E; i < 15; ++i)
        wb[i] = __builtin_amdgcn_raw_buffer_load_b128(i < 12 ? ro : rd, (4 * hw + (lane >> 4)) * (D * 2) + (lane & 15) * 16, (i < 12 ? i : i - 12) * (16 * D * 2), AUX_W2);
    }
    BAR();
    if (sub >= 3) {
      *(LAS v4u*)(L + L_W2 + (240 + 4 * hw + (lane >> 4)) * W2_ROW + (lane & 15) * 16) = v4u{0u, 0u, 0u, 0u};
#pragma unroll
      for (int i = W2E; i < 15; ++i) *(LAS v4u*)(L + L_W2 + (16 * i + 4 * hw + (lane >> 4)) * W2_ROW + (lane & 15) * 16) = wb[i];
      lds_arrive(L + L_FW + hw * 4);
      ST(12);
      {
        v4f ov[2];
        get4<2, 0>(rw, L48, G_O + (h * 8 + 2 * hw) * 48 + lane, 48, tag, ov);
        const v4f o = ov[0] + ov[1];
        if (lane < 48) *(LAS v2u*)(L + L_AOP + hw * AOP_ROW + lane * 8) = v2u{bf_pack(o[0], o[1]), bf_pack(o[2], o[3])};
      }
      BAR();
    }
  }
  if (w == 0) STW(0, 7);
  if (w == 8) { STW(8, 12); STW(14, 14); }
  if (w == 5) STW(13, 13);
}

extern "C" void kernel_launch(void* const* d_in, const int* in_sizes, int n_in, void* d_out, int out_size, void* d_ws, size_t ws_size,
                              hipStream_t stream) {
  fused_decode<<<NWG, NT, 0, stream>>>((const u16*)d_in[0], (const u16*)d_in[1], (const u16*)d_in[2], (const u16*)d_in[3], (const u16*)d_in[4],
                                       (const u16*)d_in[6], (const u16*)d_in[7], (const u16*)d_in[8], (u16*)d_out, (u64*)d_ws);
}
```
